```python
import torch

def _softmax(x, axis=-1):
    return torch.softmax(x, dim=axis)
def _gelu(x):
    return torch.nn.functional.gelu(x, approximate="tanh")
def _silu(x):
    return torch.nn.functional.silu(x)
def _sigmoid(x):
    return torch.sigmoid(x)
def _layernorm(x, axis=-1, eps=1e-5):
    m = x.mean(dim=axis, keepdim=True)
    v = x.var(dim=axis, keepdim=True, unbiased=False)
    return (x - m) / torch.sqrt(v + eps)
def _scatter_add(tgt, idx, src, axis=0):
    return tgt.clone().index_add_(axis, idx, src)
def _segment_sum(src, seg, n):
    out = torch.zeros((n,) + src.shape[1:], dtype=src.dtype)
    return out.index_add_(0, seg, src)
def _pad(x, pw):
    flat = tuple(v for pair in reversed(pw) for v in pair)
    return torch.nn.functional.pad(x, flat)

kernel_name = 'parallel_sum__grouped_kv_mqa__dense_activation_relu__prompt'

def setup_inputs():

    _w = (lambda a: (lambda t: t.to(torch.bfloat16) if t.is_floating_point() else t)(torch.as_tensor(a)))
    def _A_setup():
        B, S, D, H = 1, 2048, 512, 32
        Dh = D // H
        return {
            "x": _w((lambda *s: torch.randn(*s, dtype=torch.bfloat16) * 0.05)(B, S, D)),
            "wq": _w((lambda *s: torch.randn(*s, dtype=torch.bfloat16) * 0.2)(D, H * Dh)),
            "wk": _w((lambda *s: torch.randn(*s, dtype=torch.bfloat16) * 0.2)(D, Dh)),
            "wv": _w((lambda *s: torch.randn(*s, dtype=torch.bfloat16) * 0.1)(D, Dh)),
            "wo": _w((lambda *s: torch.randn(*s, dtype=torch.bfloat16) * 0.1)(H * Dh, D)),
        }
    _A_in = _A_setup()

    def _B_setup():
        B, S, D, Dff = 1, 2048, 512, 1376
        return {
            "x": _w((lambda *s: torch.randn(*s, dtype=torch.bfloat16) * 0.05)(B, S, D)),
            "w1": _w((lambda *s: torch.randn(*s, dtype=torch.bfloat16) * 0.1)(D, Dff)),
            "w2": _w((lambda *s: torch.randn(*s, dtype=torch.bfloat16) * 0.1)(Dff, D)),
        }
    _B_in = _B_setup()

    return {
        "A_x": _A_in["x"],
        "A_wq": _A_in["wq"],
        "A_wk": _A_in["wk"],
        "A_wv": _A_in["wv"],
        "A_wo": _A_in["wo"],
        "B_x": _B_in["x"],
        "B_w1": _B_in["w1"],
        "B_w2": _B_in["w2"],
    }

def reference(A_x, A_wq, A_wk, A_wv, A_wo, B_x, B_w1, B_w2):
    def _A_ref(x, wq, wk, wv, wo):
        B, S, D = x.shape
        H = 32
        Dh = D // H
        q = (lambda x, *d: x.permute(*d))((x @ wq).reshape(B, S, H, Dh), 0, 2, 1, 3)
        k = x @ wk
        v = x @ wv
        scores = torch.einsum("bhqd,bkd->bhqk", q, k) * (Dh ** -0.5)
        p = _softmax(scores, axis=-1)
        o = torch.einsum("bhqk,bkd->bhqd", p, v)
        return (lambda x, *d: x.permute(*d))(o, 0, 2, 1, 3).reshape(B, S, H * Dh) @ wo

    def _B_ref(x, w1, w2):
        h = torch.einsum("bsd,df->bsf", x, w1)
        zero = torch.zeros_like(h)
        h = torch.maximum(h, zero)
        return torch.einsum("bsf,fd->bsd", h, w2)

    x = A_x
    xn = _layernorm(x, axis=-1)
    return x + _A_ref(xn, A_wq, A_wk, A_wv, A_wo) + _B_ref(xn, B_w1, B_w2)
```

```cpp
#include <hip/hip_runtime.h>
#include <hip/hip_bf16.h>
typedef __hip_bfloat16 bf16;
#define S 2048
#define D 512
#define H 32
#define DH 16
#define FF 1376
static __device__ float b2f(bf16 x) { return __bfloat162float(x); }

__global__ void ln_k(const bf16* x, float* xn) {
    int row = blockIdx.x; int t = threadIdx.x;
    __shared__ float red[256];
    float a = b2f(x[row * D + t]), b = b2f(x[row * D + t + 256]);
    red[t] = a + b; __syncthreads();
    for (int s = 128; s > 0; s >>= 1) { if (t < s) red[t] += red[t + s]; __syncthreads(); }
    float m = red[0] / D; __syncthreads();
    red[t] = (a - m) * (a - m) + (b - m) * (b - m); __syncthreads();
    for (int s = 128; s > 0; s >>= 1) { if (t < s) red[t] += red[t + s]; __syncthreads(); }
    float r = 1.0f / sqrtf(red[0] / D + 1e-5f);
    xn[row * D + t] = b2f(__float2bfloat16((a - m) * r)); xn[row * D + t + 256] = b2f(__float2bfloat16((b - m) * r));
}
__global__ void gemm_k(const float* A, const bf16* W, float* C, int K, int N, int relu) {
    int row = blockIdx.y; int n = blockIdx.x * 256 + threadIdx.x; if (n >= N) return;
    float acc = 0; for (int k = 0; k < K; ++k) acc += A[row * K + k] * b2f(W[k * N + n]);
    if (relu) acc = fmaxf(acc, 0.f);
    C[row * N + n] = b2f(__float2bfloat16(acc));
}
__global__ void attn_k(const float* q, const float* k, const float* v, float* o) {
    int idx = blockIdx.x * 256 + threadIdx.x; int tok = idx / H, h = idx % H;
    float qq[DH]; for (int d = 0; d < DH; ++d) qq[d] = q[tok * D + h * DH + d];
    float m = -1e30f, l = 0; float acc[DH]; for (int d = 0; d < DH; ++d) acc[d] = 0;
    for (int j = 0; j < S; ++j) {
        float s = 0; for (int d = 0; d < DH; ++d) s += qq[d] * k[j * DH + d];
        s *= 0.25f;
        if (s > m) { float f = __expf(m - s); l *= f; for (int d = 0; d < DH; ++d) acc[d] *= f; m = s; }
        float p = __expf(s - m); l += p; for (int d = 0; d < DH; ++d) acc[d] += p * v[j * DH + d];
    }
    for (int d = 0; d < DH; ++d) o[tok * D + h * DH + d] = b2f(__float2bfloat16(acc[d] / l));
}
__global__ void fin_k(const bf16* x, const float* a, const float* b, bf16* out) {
    int i = blockIdx.x * 256 + threadIdx.x; out[i] = __float2bfloat16(b2f(x[i]) + a[i] + b[i]);
}
extern "C" void kernel_launch(void* const* d_in, const int* in_sizes, int n_in, void* d_out, int out_size, void* d_ws, size_t ws_size, hipStream_t stream) {
    const bf16* x = (const bf16*)d_in[0]; const bf16* wq = (const bf16*)d_in[1]; const bf16* wk = (const bf16*)d_in[2]; const bf16* wv = (const bf16*)d_in[3];
    const bf16* wo = (const bf16*)d_in[4]; const bf16* w1 = (const bf16*)d_in[6]; const bf16* w2 = (const bf16*)d_in[7];
    float* ws = (float*)d_ws; float* xn = ws; float* q = xn + S * D; float* k = q + S * D; float* v = k + S * DH; float* o = v + S * DH; float* a = o + S * D; float* h = a + S * D; float* b = h + S * FF;
    ln_k<<<S, 256, 0, stream>>>(x, xn);
    gemm_k<<<dim3(2, S), 256, 0, stream>>>(xn, wq, q, D, D, 0);
    gemm_k<<<dim3(1, S), 256, 0, stream>>>(xn, wk, k, D, DH, 0);
    gemm_k<<<dim3(1, S), 256, 0, stream>>>(xn, wv, v, D, DH, 0);
    attn_k<<<S * H / 256, 256, 0, stream>>>(q, k, v, o);
    gemm_k<<<dim3(2, S), 256, 0, stream>>>(o, wo, a, D, D, 0);
    gemm_k<<<dim3(6, S), 256, 0, stream>>>(xn, w1, h, D, FF, 1);
    gemm_k<<<dim3(2, S), 256, 0, stream>>>(h, w2, b, FF, D, 0);
    fin_k<<<S * D / 256, 256, 0, stream>>>(x, a, b, (bf16*)d_out);
}
```

```cpp
#include <hip/hip_runtime.h>
#include <hip/hip_bf16.h>
#include <stdint.h>

#define GAS __attribute__((address_space(1)))
#define LAS __attribute__((address_space(3)))
typedef __bf16 bf16x8 __attribute__((ext_vector_type(8)));
typedef __bf16 bf16x16 __attribute__((ext_vector_type(16)));
typedef short s16x4 __attribute__((ext_vector_type(4)));
typedef float f32x4 __attribute__((ext_vector_type(4)));
typedef float f32x8 __attribute__((ext_vector_type(8)));
typedef float f32x16 __attribute__((ext_vector_type(16)));
typedef unsigned u32x2 __attribute__((ext_vector_type(2)));
typedef unsigned u32x4 __attribute__((ext_vector_type(4)));
typedef unsigned long long u64;
typedef GAS u64 gu64;

constexpr int DM = 512, FF = 1376;
constexpr int NKS2 = FF / 16;
constexpr unsigned WS_E = 0;
constexpr unsigned WS_FKV = 4096;
constexpr unsigned WS_FS1 = 8192;
constexpr unsigned WS_FO = WS_FS1 + 256 * 128;
constexpr unsigned WS_KF = 131072;
constexpr unsigned WS_VF = WS_KF + 65536;
constexpr unsigned WS_W2F = WS_VF + 65536;
constexpr unsigned WS_WOF = WS_W2F + 8 * 2 * NKS2 * 1024;
constexpr unsigned WS_HF = WS_WOF + 8 * 2 * 32 * 1024;
constexpr unsigned WS_OF = WS_HF + 32 * 2 * NKS2 * 1024;
constexpr unsigned WS_END = WS_OF + 32 * 2 * 32 * 1024;
constexpr int L_XN = 0;
constexpr int L_WB = 65536;
constexpr int L_K = 0;
constexpr int L_V = 65536;
constexpr int L_QF = L_V + 256 * 272;
constexpr int L_RED = 0;
constexpr int LDS_BYTES = 163840;
static_assert(L_QF >= L_WB + 2 * 32768 && L_QF + 8192 <= LDS_BYTES, "QF sits in staging buffer 2");

constexpr float QSCALE = 0.25f * 1.4426950408889634f;
constexpr float MBIAS = 80.0f;
constexpr int SC1 = 16;

#define RLX_AGENT __ATOMIC_RELAXED, __HIP_MEMORY_SCOPE_AGENT
#define MFMA32(a, b, c) __builtin_amdgcn_mfma_f32_32x32x16_bf16(a, b, c, 0, 0, 0)
#define MFMA16(a, b, c) __builtin_amdgcn_mfma_f32_16x16x32_bf16(a, b, c, 0, 0, 0)
#define BARRIER() asm volatile("s_waitcnt lgkmcnt(0)\n\ts_barrier" ::: "memory")
#define WAIT_VM(n) asm volatile("s_waitcnt vmcnt(" #n ")" ::: "memory")

__device__ __forceinline__ unsigned cvt_pk(float lo, float hi) {
    typedef __bf16 b2 __attribute__((ext_vector_type(2)));
    typedef float f2 __attribute__((ext_vector_type(2)));
    f2 v = {lo, hi};
    return __builtin_bit_cast(unsigned, __builtin_convertvector(v, b2));
}
__device__ __forceinline__ float bf_lo(unsigned u) { return __uint_as_float(u << 16); }
__device__ __forceinline__ float bf_hi(unsigned u) { return __uint_as_float(u & 0xffff0000u); }
__device__ __forceinline__ void glds16(const void* g, unsigned lds_base) {
    unsigned sv;
    asm volatile("s_mov_b32 %0, m0\n\ts_mov_b32 m0, %2\n\ts_nop 0\n\tglobal_load_lds_dwordx4 %1, off\n\ts_mov_b32 m0, %0"
                 : "=&s"(sv) : "v"(g), "s"(lds_base) : "memory");
}
__device__ __forceinline__ bf16x8 lds_ld16(const LAS char* p) { return *(const LAS bf16x8*)p; }
__device__ __forceinline__ s16x4 lds_tr(const LAS char* p) {
    return __builtin_amdgcn_ds_read_tr16_b64_v4i16((LAS s16x4*)p);
}
__device__ __forceinline__ bf16x8 tr_frag(const LAS char* p0, const LAS char* p1) {
    s16x4 a = lds_tr(p0), b = lds_tr(p1);
    typedef short s16x8 __attribute__((ext_vector_type(8)));
    s16x8 v = __builtin_shufflevector(a, b, 0, 1, 2, 3, 4, 5, 6, 7);
    return __builtin_bit_cast(bf16x8, v);
}
__device__ __forceinline__ void wait_flags(gu64* word, u64 tag, bool active) {
    for (unsigned spins = 0; spins < (1u << 22); ++spins) {
        bool ok = true;
        if (active) ok = (__hip_atomic_load(word, RLX_AGENT) == tag);
        if (__all(ok)) break;
        __builtin_amdgcn_s_sleep(1);
    }
}

struct Params {
    const unsigned short* x; const unsigned short* wq; const unsigned short* wk; const unsigned short* wv; const unsigned short* wo;
    const unsigned short* w1; const unsigned short* w2; unsigned short* out; char* ws;
};

__global__ void __launch_bounds__(512, 2) fused_block_kernel(Params P) {
    __shared__ __attribute__((aligned(16))) char lds_raw[LDS_BYTES];
    LAS char* lds = (LAS char*)lds_raw;
    const unsigned lds0 = (unsigned)(uintptr_t)lds;
    const int tid = threadIdx.x, lane = tid & 63;
    const int w = __builtin_amdgcn_readfirstlane(tid >> 6);
    const int b = blockIdx.x;
    const int p = (b & 7) * 4 + ((b >> 3) & 3);
    const int c = b >> 5;
    const bool producer = (c == (p & 7));
    const int r32 = lane & 31, h = lane >> 5;
    char* ws = P.ws;
    const __amdgpu_buffer_rsrc_t wsr = __builtin_amdgcn_make_buffer_rsrc(ws, 0, WS_END, 0x00020000);

    u64 tag = 0;
    if (w == 0) { tag = __hip_atomic_load((gu64*)(ws + WS_E), RLX_AGENT) + 1; }

    const int ft0 = c < 3 ? 6 * c : 18 + 5 * (c - 3);
    const int nft = c < 3 ? 6 : 5;

    const int u = w >> 1, th = w & 1;
    const char* wsrc; unsigned wstride;
    {
        const int rl = lane >> 3;
        int piece = (lane & 7) ^ (((rl >> 1) & 1) << 2);
        if (u == 3 && ft0 + 5 >= FF / 32) piece &= 3;
        if (u == 0) { wsrc = (const char*)P.wq + c * 128; wstride = 1024; }
        else { wsrc = (const char*)P.w1 + ft0 * 64 + (u - 1) * 128; wstride = FF * 2; }
        wsrc += (size_t)(32 * th + rl) * wstride + piece * 16;
    }
    const unsigned wdst = lds0 + L_WB + u * 8192 + th * 4096;
    auto stage_chunk = [&](int kc, int buf) {
#pragma unroll
        for (int i = 0; i < 4; ++i)
            glds16(wsrc + (size_t)(64 * kc + 8 * i) * wstride, __builtin_amdgcn_readfirstlane(wdst + buf * 32768 + i * 1024));
    };
    stage_chunk(0, 0);
    stage_chunk(1, 1);
    if (producer) {
        const char* src = (w < 4 ? (const char*)P.wk : (const char*)P.wv) + (w & 3) * 4096 + lane * 16;
#pragma unroll
        for (int i = 0; i < 4; ++i) glds16(src + i * 1024, __builtin_amdgcn_readfirstlane(lds0 + L_WB + 2 * 32768 + w * 4096 + i * 1024));
    }
    const int rl_idx = w >> 1, rl_nt = w & 1;
    const int rl_s = rl_idx < 3 ? p + 32 * rl_idx : p;
    const bool rl_on = rl_idx == 3 || rl_s < NKS2;
    unsigned short rl_v[8];
    if (rl_on) {
        const unsigned short* src = (rl_idx < 3 ? P.w2 : P.wo) + (size_t)(16 * rl_s + 4 * h) * DM + 64 * c + 32 * rl_nt + r32;
#pragma unroll
        for (int j = 0; j < 8; ++j) rl_v[j] = src[(8 * (j >> 2) + (j & 3)) * DM];
    }
    {
        const int tk = 8 * w + (lane >> 3), part = lane & 7;
        const u32x4* xr = (const u32x4*)(P.x + (size_t)(64 * p + tk) * DM) + part;
        u32x4 xv[8];
#pragma unroll
        for (int i = 0; i < 8; ++i) xv[i] = xr[8 * i];
        float f[64];
        float s = 0.f;
#pragma unroll
        for (int i = 0; i < 8; ++i)
#pragma unroll
            for (int j = 0; j < 4; ++j) { f[8 * i + 2 * j] = bf_lo(xv[i][j]); f[8 * i + 2 * j + 1] = bf_hi(xv[i][j]); }
#pragma unroll
        for (int i = 0; i < 64; ++i) s += f[i];
        s += __shfl_xor(s, 1); s += __shfl_xor(s, 2); s += __shfl_xor(s, 4);
        const float mean = s * (1.0f / DM);
        float q = 0.f;
#pragma unroll
        for (int i = 0; i < 64; ++i) { f[i] -= mean; q += f[i] * f[i]; }
        q += __shfl_xor(q, 1); q += __shfl_xor(q, 2); q += __shfl_xor(q, 4);
        const float rstd = 1.0f / sqrtf(q * (1.0f / DM) + 1e-5f);
        const int tth = tk >> 5, tr = tk & 31;
#pragma unroll
        for (int i = 0; i < 8; ++i) {
            const int ch = part + 8 * i;
            u32x4 o;
#pragma unroll
            for (int j = 0; j < 4; ++j) o[j] = cvt_pk(f[8 * i + 2 * j] * rstd, f[8 * i + 2 * j + 1] * rstd);
            *(LAS u32x4*)(lds + L_XN + tth * 32768 + ch * 512 + ((tr ^ part) << 4)) = o;
        }
    }
    if (rl_on) {
        u32x4 o;
#pragma unroll
        for (int j = 0; j < 4; ++j) o[j] = (unsigned)rl_v[2 * j] | ((unsigned)rl_v[2 * j + 1] << 16);
        const unsigned off = (rl_idx < 3 ? WS_W2F + ((c * 2 + rl_nt) * NKS2 + rl_s) * 1024 : WS_WOF + ((c * 2 + rl_nt) * 32 + rl_s) * 1024) + lane * 16;
        __builtin_amdgcn_raw_buffer_store_b128(o, wsr, off, 0, SC1);
    }
    WAIT_VM(0);
    BARRIER();
    if (producer) {
        const int grp = w >> 1, kind = w & 1, g = lane >> 4, i16 = lane & 15;
        const LAS char* wp = lds + L_WB + 2 * 32768 + kind * 16384 + (8 * g + (i16 >> 2)) * 32 + (i16 & 3) * 8;
        const int tr = (grp & 1) * 16 + i16;
        const LAS char* xp0 = lds + L_XN + (grp >> 1) * 32768 + g * 512 + ((tr ^ g) << 4);
        const LAS char* xp1 = lds + L_XN + (grp >> 1) * 32768 + (4 + g) * 512 + ((tr ^ (4 + g)) << 4);
        f32x4 acc = {0.f, 0.f, 0.f, 0.f};
#pragma unroll
        for (int s = 0; s < 16; ++s) {
            bf16x8 wf = tr_frag(wp + s * 1024, wp + s * 1024 + 128);
            bf16x8 xf = lds_ld16(((s & 1) ? xp1 : xp0) + (s >> 1) * 4096);
            acc = kind == 0 ? MFMA16(wf, xf, acc) : MFMA16(xf, wf, acc);
        }
        u32x2 o = {cvt_pk(acc[0], acc[1]), cvt_pk(acc[2], acc[3])};
        const int t = 2 * p + (grp >> 1);
        const unsigned off = kind == 0 ? WS_KF + t * 1024 + (g & 1) * 512 + tr * 16 + (g >> 1) * 8
                                       : WS_VF + t * 1024 + (grp & 1) * 512 + (g >> 1) * 256 + i16 * 16 + (g & 1) * 8;
        __builtin_amdgcn_raw_buffer_store_b64(o, wsr, off, 0, SC1);
        WAIT_VM(0);
    }
    f32x16 acc0 = {}, acc1 = {};
    {
        const int G = lane >> 4, i16 = lane & 15, q4 = i16 >> 2, swz = (q4 >> 1) & 1;
        const int abase = L_WB + u * 8192 + (8 * (G >> 1) + q4) * 128 + (G & 1) * 32 + (i16 & 3) * 8;
        const LAS char* a0 = lds + abase + swz * 64;
        const LAS char* a1 = lds + abase + (swz ^ 1) * 64;
        const LAS char* bb[4];
#pragma unroll
        for (int s = 0; s < 4; ++s) bb[s] = lds + L_XN + th * 32768 + (2 * s + h) * 512 + ((r32 ^ (2 * s + h)) << 4);
#pragma unroll
        for (int kc = 0; kc < 8; ++kc) {
            if (kc > 0) { if (kc < 7) WAIT_VM(4); else WAIT_VM(0); }
            BARRIER();
            if (kc == 0 && producer && tid == 0) __hip_atomic_store((gu64*)(ws + WS_FKV + p * 128), tag, RLX_AGENT);
            if (kc + 2 < 8) stage_chunk(kc + 2, (kc + 2) % 3);
            const int bo = (kc % 3) * 32768;
#pragma unroll
            for (int s = 0; s < 4; ++s) {
                bf16x8 fa0 = tr_frag(a0 + bo + s * 2048, a0 + bo + s * 2048 + 512);
                bf16x8 fa1 = tr_frag(a1 + bo + s * 2048, a1 + bo + s * 2048 + 512);
                bf16x8 fb = lds_ld16(bb[s] + kc * 4096);
                acc0 = MFMA32(fa0, fb, acc0);
                acc1 = MFMA32(fa1, fb, acc1);
            }
        }
    }
    if (u == 0) {
#pragma unroll
        for (int hl = 0; hl < 4; ++hl) {
            const f32x16& a = hl < 2 ? acc0 : acc1;
            const int o8 = (hl & 1) * 8;
            u32x4 o;
#pragma unroll
            for (int j = 0; j < 4; ++j) o[j] = cvt_pk(a[o8 + 2 * j] * QSCALE, a[o8 + 2 * j + 1] * QSCALE);
            *(LAS u32x4*)(lds + L_QF + (hl * 2 + th) * 1024 + lane * 16) = o;
        }
    } else {
#pragma unroll
        for (int ft = 0; ft < 2; ++ft) {
            const f32x16& a = ft == 0 ? acc0 : acc1;
            const int lt = 2 * (u - 1) + ft;
            if (lt < nft) {
#pragma unroll
                for (int s = 0; s < 2; ++s) {
                    u32x4 o;
#pragma unroll
                    for (int j = 0; j < 4; ++j) o[j] = cvt_pk(fmaxf(a[8 * s + 2 * j], 0.f), fmaxf(a[8 * s + 2 * j + 1], 0.f));
                    __builtin_amdgcn_raw_buffer_store_b128(o, wsr, WS_HF + ((p * 2 + th) * NKS2 + 2 * (ft0 + lt) + s) * 1024 + lane * 16, 0, SC1);
                }
            }
        }
    }
    WAIT_VM(0);
    BARRIER();
    if (w == 0) {
        if (lane == 0) __hip_atomic_store((gu64*)(ws + WS_FS1 + b * 128), tag, RLX_AGENT);
        wait_flags((gu64*)(ws + WS_FKV + r32 * 128), tag, lane < 32);
    }
    BARRIER();
    {
        u32x4 kv[16];
#pragma unroll
        for (int i = 0; i < 16; ++i) kv[i] = __builtin_amdgcn_raw_buffer_load_b128(wsr, WS_KF + (w * 16 + i) * 1024 + lane * 16, 0, SC1);
        if (tid < 256) *(LAS u32x4*)(lds + L_V + tid * 272 + 256) = (u32x4){0x3F803F80u, 0x3F803F80u, 0x3F803F80u, 0x3F803F80u};
#pragma unroll
        for (int i = 0; i < 16; ++i) {
            const int kb = w * 16 + i;
            if (w < 4) *(LAS u32x4*)(lds + L_K + kb * 1024 + lane * 16) = kv[i];
            else *(LAS u32x4*)(lds + L_V + ((kb - 64) * 4 + (lane >> 4)) * 272 + (lane & 15) * 16) = kv[i];
        }
    }
    BARRIER();
    {
        const bf16x8 qf = lds_ld16(lds + L_QF + w * 1024 + lane * 16);
        const LAS char* kp = lds + L_K + lane * 16;
        const LAS char* vp = lds + L_V + h * 272 + (r32 < 16 ? r32 : 16) * 16;
        const int sidx = 0xDDDD;
        float on[8];
        for (int attempt = 0; attempt < 2; ++attempt) {
            const int nmax = attempt == 0 ? 2 : 64;
            float mx = -3.0e38f;
            for (int t = 0; t < nmax; ++t) {
                f32x16 z = {};
                f32x16 s = MFMA32(lds_ld16(kp + t * 1024), qf, z);
#pragma unroll
                for (int j = 0; j < 16; j += 2) mx = fmaxf(mx, fmaxf(s[j], s[j + 1]));
            }
            mx = fmaxf(mx, __shfl_xor(mx, 32));
            const float nm = -(mx + (attempt == 0 ? MBIAS : 0.f));
            f32x16 negm;
#pragma unroll
            for (int j = 0; j < 16; ++j) negm[j] = nm;
            f32x16 acc = {};
            f32x16 sc = MFMA32(lds_ld16(kp), qf, negm);
#pragma unroll 2
            for (int t = 0; t < 64; ++t) {
                const bf16x8 kn = lds_ld16(kp + ((t + 1) & 63) * 1024);
                const bf16x8 v0 = lds_ld16(vp + t * 1088), v1 = lds_ld16(vp + t * 1088 + 544);
                const f32x16 sn = MFMA32(kn, qf, negm);
                f32x8 p0, p1;
#pragma unroll
                for (int j = 0; j < 8; ++j) p0[j] = __builtin_amdgcn_exp2f(sc[j]);
                acc = __builtin_amdgcn_smfmac_f32_32x32x32_bf16(v0, __builtin_bit_cast(bf16x16, p0), acc, sidx, 0, 0);
#pragma unroll
                for (int j = 0; j < 8; ++j) p1[j] = __builtin_amdgcn_exp2f(sc[8 + j]);
                acc = __builtin_amdgcn_smfmac_f32_32x32x32_bf16(v1, __builtin_bit_cast(bf16x16, p1), acc, sidx, 0, 0);
                sc = sn;
            }
            const float l = acc[8];
            const float inv = 1.0f / l;
            bool ok = (l > 1.0e-30f) && (l < 1.0e37f);
#pragma unroll
            for (int j = 0; j < 8; ++j) { on[j] = acc[j] * inv; ok = ok && (fabsf(acc[j]) < 3.0e38f); }
            if (__all(ok)) break;
        }
        u32x4 o;
#pragma unroll
        for (int j = 0; j < 4; ++j) o[j] = cvt_pk(on[2 * j], on[2 * j + 1]);
        __builtin_amdgcn_raw_buffer_store_b128(o, wsr, WS_OF + ((p * 2 + (w & 1)) * 32 + 4 * c + (w >> 1)) * 1024 + lane * 16, 0, SC1);
    }
    WAIT_VM(0);
    BARRIER();
    if (w == 0) {
        if (lane == 0) __hip_atomic_store((gu64*)(ws + WS_FO + b * 128), tag, RLX_AGENT);
        if (b == 0) {
#pragma unroll
            for (int i = 0; i < 4; ++i) wait_flags((gu64*)(ws + WS_FS1 + (lane + 64 * i) * 128), tag, true);
        } else {
            const int pp = lane < 32 ? lane : p, cc = lane < 32 ? c : (lane & 7);
            const int bb = (cc << 5) | ((pp & 3) << 3) | (pp >> 2);
            wait_flags((gu64*)(ws + WS_FS1 + bb * 128), tag, lane < 40);
        }
    }
    BARRIER();
    {
        const int tth = w & 1, kq = w >> 1;
        f32x16 y0 = {}, y1 = {};
        const int s0 = kq < 2 ? 22 * kq : 44 + 21 * (kq - 2), ns = kq < 2 ? 22 : 21;
        const unsigned ao0 = WS_W2F + (c * 2 + 0) * NKS2 * 1024 + lane * 16, ao1 = ao0 + NKS2 * 1024;
        const unsigned bo = WS_HF + (p * 2 + tth) * NKS2 * 1024 + lane * 16;
        for (int s = 0; s < ns; s += 7) {
            bf16x8 fa0[7], fa1[7], fb[7];
#pragma unroll
            for (int i = 0; i < 7; ++i) {
                const int ss = s0 + (s + i < ns ? s + i : ns - 1);
                fa0[i] = __builtin_bit_cast(bf16x8, __builtin_amdgcn_raw_buffer_load_b128(wsr, ao0 + ss * 1024, 0, SC1));
                fa1[i] = __builtin_bit_cast(bf16x8, __builtin_amdgcn_raw_buffer_load_b128(wsr, ao1 + ss * 1024, 0, SC1));
                fb[i] = __builtin_bit_cast(bf16x8, __builtin_amdgcn_raw_buffer_load_b128(wsr, bo + ss * 1024, 0, SC1));
            }
#pragma unroll
            for (int i = 0; i < 7; ++i)
                if (s + i < ns) { y0 = MFMA32(fa0[i], fb[i], y0); y1 = MFMA32(fa1[i], fb[i], y1); }
        }
        bf16x8 ga0[8], ga1[8], gb[8];
        const unsigned wo0 = WS_WOF + ((c * 2 + 0) * 32 + 8 * kq) * 1024 + lane * 16, wo1 = wo0 + 32 * 1024;
#pragma unroll
        for (int i = 0; i < 8; ++i) {
            ga0[i] = __builtin_bit_cast(bf16x8, __builtin_amdgcn_raw_buffer_load_b128(wsr, wo0 + i * 1024, 0, SC1));
            ga1[i] = __builtin_bit_cast(bf16x8, __builtin_amdgcn_raw_buffer_load_b128(wsr, wo1 + i * 1024, 0, SC1));
        }
        const int fnt = kq >> 1, fq = 2 * (kq & 1);
        const size_t orow = (size_t)(64 * p + 32 * tth + r32) * DM + 64 * c + 32 * fnt + 8 * fq;
        const u32x2 xr0 = *(const u32x2*)(P.x + orow + 4 * h), xr1 = *(const u32x2*)(P.x + orow + 8 + 4 * h);
        if (w == 0) {
            const int bb = ((lane & 7) << 5) | ((p & 3) << 3) | (p >> 2);
            wait_flags((gu64*)(ws + WS_FO + bb * 128), tag, lane < 8);
        }
        BARRIER();
        const unsigned oo = WS_OF + ((p * 2 + tth) * 32 + 8 * kq) * 1024 + lane * 16;
#pragma unroll
        for (int i = 0; i < 8; ++i) gb[i] = __builtin_bit_cast(bf16x8, __builtin_amdgcn_raw_buffer_load_b128(wsr, oo + i * 1024, 0, SC1));
#pragma unroll
        for (int i = 0; i < 8; ++i) { y0 = MFMA32(ga0[i], gb[i], y0); y1 = MFMA32(ga1[i], gb[i], y1); }
        LAS char* mine = lds + L_RED + w * 8192 + lane * 16;
#pragma unroll
        for (int q = 0; q < 4; ++q) {
            *(LAS f32x4*)(mine + q * 1024) = (f32x4){y0[4 * q], y0[4 * q + 1], y0[4 * q + 2], y0[4 * q + 3]};
            *(LAS f32x4*)(mine + 4096 + q * 1024) = (f32x4){y1[4 * q], y1[4 * q + 1], y1[4 * q + 2], y1[4 * q + 3]};
        }
        BARRIER();
        f32x4 z0 = {0.f, 0.f, 0.f, 0.f}, z1 = z0;
#pragma unroll
        for (int k = 0; k < 4; ++k) {
            const LAS char* src = lds + L_RED + (2 * k + tth) * 8192 + fnt * 4096 + fq * 1024 + lane * 16;
            z0 += *(const LAS f32x4*)src; z1 += *(const LAS f32x4*)(src + 1024);
        }
        z0[0] += bf_lo(xr0[0]); z0[1] += bf_hi(xr0[0]); z0[2] += bf_lo(xr0[1]); z0[3] += bf_hi(xr0[1]);
        z1[0] += bf_lo(xr1[0]); z1[1] += bf_hi(xr1[0]); z1[2] += bf_lo(xr1[1]); z1[3] += bf_hi(xr1[1]);
        unsigned a0 = cvt_pk(z0[0], z0[1]), a1 = cvt_pk(z0[2], z0[3]), b0 = cvt_pk(z1[0], z1[1]), b1 = cvt_pk(z1[2], z1[3]);
        auto r0 = __builtin_amdgcn_permlane32_swap(a0, b0, false, false);
        auto r1 = __builtin_amdgcn_permlane32_swap(a1, b1, false, false);
        *(u32x4*)(P.out + orow + 8 * h) = (u32x4){r0[0], r1[0], r0[1], r1[1]};
    }
    if (b == 0 && tid == 0) __hip_atomic_store((gu64*)(ws + WS_E), tag, RLX_AGENT);
}

extern "C" void kernel_launch(void* const* d_in, const int* in_sizes, int n_in, void* d_out, int out_size,
                              void* d_ws, size_t ws_size, hipStream_t stream) {
    Params P;
    P.x = (const unsigned short*)d_in[0]; P.wq = (const unsigned short*)d_in[1]; P.wk = (const unsigned short*)d_in[2];
    P.wv = (const unsigned short*)d_in[3]; P.wo = (const unsigned short*)d_in[4];
    P.w1 = (const unsigned short*)d_in[6]; P.w2 = (const unsigned short*)d_in[7];
    P.out = (unsigned short*)d_out; P.ws = (char*)d_ws;
    fused_block_kernel<<<256, 512, 0, stream>>>(P);
}
```

```cpp
#include <hip/hip_runtime.h>
#include <hip/hip_bf16.h>
#include <stdint.h>

#define GAS __attribute__((address_space(1)))
#define LAS __attribute__((address_space(3)))
typedef __bf16 bf16x8 __attribute__((ext_vector_type(8)));
typedef short s16x4 __attribute__((ext_vector_type(4)));
typedef float f32x2 __attribute__((ext_vector_type(2)));
typedef float f32x4 __attribute__((ext_vector_type(4)));
typedef float f32x16 __attribute__((ext_vector_type(16)));
typedef unsigned u32x2 __attribute__((ext_vector_type(2)));
typedef unsigned u32x4 __attribute__((ext_vector_type(4)));
typedef unsigned long long u64;
typedef GAS u64 gu64;

constexpr int DM = 512, FF = 1376;
constexpr int NKS2 = FF / 16;
constexpr unsigned WS_E = 0;
constexpr unsigned WS_FKV = 4096;
constexpr unsigned WS_FS1 = WS_FKV + 512 * 128;
constexpr unsigned WS_FO = WS_FS1 + 256 * 128;
constexpr unsigned WS_KF = 524288;
constexpr unsigned WS_VF = WS_KF + 65536;
constexpr unsigned WS_W2F = WS_VF + 65536;
constexpr unsigned WS_WOF = WS_W2F + 8 * 2 * NKS2 * 1024;
constexpr unsigned WS_HF = WS_WOF + 8 * 2 * 32 * 1024;
constexpr unsigned WS_OF = WS_HF + 32 * 2 * NKS2 * 1024;
constexpr unsigned WS_END = WS_OF + 32 * 2 * 32 * 1024;
static_assert(WS_FO + 2048 * 128 <= WS_KF, "flags overlap");
constexpr int L_RING = 0;
constexpr int L_XN = 81920;
constexpr int L_E = 147456;
constexpr int TS = 2080;
constexpr int NTA = 38;
constexpr int L_QF = L_E;
constexpr int L_CNT = L_E + 8192;
constexpr int L_X0 = 64 * TS;
constexpr int L_X3 = L_CNT + 64;
constexpr int LDS_BYTES = 163840;
static_assert(L_X0 % 16 == 0 && L_X0 + 3 * 4096 <= L_E && L_X3 + 4096 <= LDS_BYTES && 14 * TS <= 32768 && 22 * TS <= 49152 && 30 * TS <= 65536 && NTA * TS <= 81920, "LDS map");

constexpr float QSCALE = 0.25f * 1.4426950408889634f;
constexpr float MBIAS = 94.0f;
constexpr int SC1 = 16;

#define RLX_AGENT __ATOMIC_RELAXED, __HIP_MEMORY_SCOPE_AGENT
#define MFMA32(a, b, c) __builtin_amdgcn_mfma_f32_32x32x16_bf16(a, b, c, 0, 0, 0)
#define MFMA16(a, b, c) __builtin_amdgcn_mfma_f32_16x16x32_bf16(a, b, c, 0, 0, 0)
#define BARRIER() asm volatile("s_waitcnt lgkmcnt(0)\n\ts_barrier" ::: "memory")
#define WAIT_VM(n) asm volatile("s_waitcnt vmcnt(%0)" :: "i"(n) : "memory")
#define SB() __builtin_amdgcn_sched_barrier(0)

__device__ __forceinline__ unsigned cvt_pk(float lo, float hi) {
    typedef __bf16 b2 __attribute__((ext_vector_type(2)));
    f32x2 v = {lo, hi};
    return __builtin_bit_cast(unsigned, __builtin_convertvector(v, b2));
}
__device__ __forceinline__ float bf_lo(unsigned u) { return __uint_as_float(u << 16); }
__device__ __forceinline__ float bf_hi(unsigned u) { return __uint_as_float(u & 0xffff0000u); }
__device__ __forceinline__ void dma16(__amdgpu_buffer_rsrc_t r, unsigned voff, unsigned soff, unsigned lds_base) {
    asm volatile("s_mov_b32 m0, %0\n\ts_nop 0\n\tbuffer_load_dwordx4 %1, %2, %3 offen lds" :: "s"(lds_base), "v"(voff), "s"(r), "s"(soff) : "memory");
}
__device__ __forceinline__ void dma16_sc1(__amdgpu_buffer_rsrc_t r, unsigned voff, unsigned soff, unsigned lds_base) {
    asm volatile("s_mov_b32 m0, %0\n\ts_nop 0\n\tbuffer_load_dwordx4 %1, %2, %3 offen sc1 lds" :: "s"(lds_base), "v"(voff), "s"(r), "s"(soff) : "memory");
}
__device__ __forceinline__ bf16x8 lds_ld16(const LAS char* p) { return *(const LAS bf16x8*)p; }
__device__ __forceinline__ bf16x8 tr_frag(const LAS char* p0, const LAS char* p1) {
    s16x4 a = __builtin_amdgcn_ds_read_tr16_b64_v4i16((LAS s16x4*)p0), b = __builtin_amdgcn_ds_read_tr16_b64_v4i16((LAS s16x4*)p1);
    typedef short s16x8 __attribute__((ext_vector_type(8)));
    s16x8 v = __builtin_shufflevector(a, b, 0, 1, 2, 3, 4, 5, 6, 7);
    return __builtin_bit_cast(bf16x8, v);
}
constexpr int KVJ = 3;
constexpr int POLLJ = 12;
constexpr int CHKJ = 15;
constexpr int SIGC = 7;
constexpr int RING = 8;
constexpr int NFFN = 43;
__device__ __forceinline__ bf16x8 ld_frag(__amdgpu_buffer_rsrc_t r, unsigned voff, unsigned soff) {
    return __builtin_bit_cast(bf16x8, __builtin_amdgcn_raw_buffer_load_b128(r, voff, soff, SC1));
}
__device__ __forceinline__ float red8(float v) {
    v += __builtin_bit_cast(float, __builtin_amdgcn_update_dpp(0, __builtin_bit_cast(int, v), 0xB1, 0xF, 0xF, true));
    v += __builtin_bit_cast(float, __builtin_amdgcn_update_dpp(0, __builtin_bit_cast(int, v), 0x4E, 0xF, 0xF, true));
    v += __builtin_bit_cast(float, __builtin_amdgcn_update_dpp(0, __builtin_bit_cast(int, v), 0x141, 0xF, 0xF, true));
    return v;
}
__device__ __forceinline__ int block_of(int p, int c) { return (((c & 3) * 8 + (p & 7)) << 3) | ((p >> 3) << 1) | (c >> 2); }
__device__ __forceinline__ void wait_flags(gu64* word, u64 tag, bool active) {
    for (unsigned spins = 0; spins < (1u << 22); ++spins) {
        bool ok = true;
        if (active) ok = (__hip_atomic_load(word, RLX_AGENT) == tag);
        if (__all(ok)) break;
        __builtin_amdgcn_s_sleep(1);
    }
}
__device__ __forceinline__ void wait_flags4(gu64* word, u64 tg) {
    for (unsigned spins = 0; spins < (1u << 22); ++spins) {
        const u64 a = __hip_atomic_load(word, RLX_AGENT), b = __hip_atomic_load(word + 64 * 16, RLX_AGENT),
                  c = __hip_atomic_load(word + 128 * 16, RLX_AGENT), d = __hip_atomic_load(word + 192 * 16, RLX_AGENT);
        if (__all(a == tg && b == tg && c == tg && d == tg)) break;
        __builtin_amdgcn_s_sleep(1);
    }
}

#define ATT_LOOP(o, q, nm_, ka_, va_, vb_, k1_, k2_, sig_, ca_) do { unsigned cnt_, st_; asm volatile( \
    "v_mov_b32 v76, %[nm]\n\t" \
    "v_mov_b32 v77, %[nm]\n\t" \
    "v_mov_b32 v78, %[nm]\n\t" \
    "v_mov_b32 v79, %[nm]\n\t" \
    "v_mov_b32 v80, %[nm]\n\t" \
    "v_mov_b32 v81, %[nm]\n\t" \
    "v_mov_b32 v82, %[nm]\n\t" \
    "v_mov_b32 v83, %[nm]\n\t" \
    "v_mov_b32 v84, %[nm]\n\t" \
    "v_mov_b32 v85, %[nm]\n\t" \
    "v_mov_b32 v86, %[nm]\n\t" \
    "v_mov_b32 v87, %[nm]\n\t" \
    "v_mov_b32 v88, %[nm]\n\t" \
    "v_mov_b32 v89, %[nm]\n\t" \
    "v_mov_b32 v90, %[nm]\n\t" \
    "v_mov_b32 v91, %[nm]\n\t" \
    "v_mov_b32 v124, %[q0]\n\t" \
    "v_mov_b32 v125, %[q1]\n\t" \
    "v_mov_b32 v126, %[q2]\n\t" \
    "v_mov_b32 v127, %[q3]\n\t" \
    "v_mov_b32 v153, %[ka]\n\t" \
    "v_mov_b32 v154, %[va]\n\t" \
    "v_mov_b32 v155, %[vb]\n\t" \
    "v_mov_b32 v152, 0xdddd\n\t" \
    "ds_read_b128 v[128:131], v153\n\t" \
    "ds_read_b128 v[132:135], v153 offset:2080\n\t" \
    "ds_read_b128 v[136:139], v154\n\t" \
    "v_mov_b32 v60, 0\n\t" \
    "v_mov_b32 v61, 0\n\t" \
    "v_mov_b32 v62, 0\n\t" \
    "v_mov_b32 v63, 0\n\t" \
    "v_mov_b32 v64, 0\n\t" \
    "v_mov_b32 v65, 0\n\t" \
    "v_mov_b32 v66, 0\n\t" \
    "v_mov_b32 v67, 0\n\t" \
    "v_mov_b32 v68, 0\n\t" \
    "v_mov_b32 v69, 0\n\t" \
    "v_mov_b32 v70, 0\n\t" \
    "v_mov_b32 v71, 0\n\t" \
    "v_mov_b32 v72, 0\n\t" \
    "v_mov_b32 v73, 0\n\t" \
    "v_mov_b32 v74, 0\n\t" \
    "v_mov_b32 v75, 0\n\t" \
    "v_mov_b32 v116, 0\n\t" \
    "v_mov_b32 v117, 0\n\t" \
    "v_mov_b32 v118, 0\n\t" \
    "v_mov_b32 v119, 0\n\t" \
    "v_mov_b32 v120, 0\n\t" \
    "v_mov_b32 v121, 0\n\t" \
    "v_mov_b32 v122, 0\n\t" \
    "v_mov_b32 v123, 0\n\t" \
    "v_mov_b32 v148, 0\n\t" \
    "v_mov_b32 v149, 0\n\t" \
    "v_mov_b32 v150, 0\n\t" \
    "v_mov_b32 v151, 0\n\t" \
    "s_waitcnt lgkmcnt(0)\n\t" \
    "v_mfma_f32_32x32x16_bf16 v[92:107], v[128:131], v[124:127], v[76:91]\n\t" \
    "s_movk_i32 %[cnt], 8\n\t" \
    "s_setprio 2\n\t" \
    "s_nop 15\n\t" \
    "1:\n\t" \
    "v_exp_f32 v92, v92\n\t" \
    "v_exp_f32 v93, v93\n\t" \
    "s_waitcnt lgkmcnt(0)\n\t" \
    "v_smfmac_f32_32x32x32_bf16 v[60:75], v[148:151], v[116:123], v152\n\t" \
    "ds_read_b128 v[144:147], v155 offset:0\n\t" \
    "ds_read_b128 v[128:131], v153 offset:4160\n\t" \
    "v_exp_f32 v94, v94\n\t" \
    "v_exp_f32 v95, v95\n\t" \
    "v_exp_f32 v96, v96\n\t" \
    "v_exp_f32 v97, v97\n\t" \
    "v_mfma_f32_32x32x16_bf16 v[108:123], v[132:135], v[124:127], v[76:91]\n\t" \
    "ds_read_b128 v[140:143], v154 offset:2080\n\t" \
    "v_exp_f32 v98, v98\n\t" \
    "v_exp_f32 v99, v99\n\t" \
    "v_exp_f32 v100, v100\n\t" \
    "v_exp_f32 v101, v101\n\t" \
    "v_exp_f32 v102, v102\n\t" \
    "v_exp_f32 v103, v103\n\t" \
    "v_smfmac_f32_32x32x32_bf16 v[60:75], v[136:139], v[92:99], v152\n\t" \
    "v_exp_f32 v104, v104\n\t" \
    "v_exp_f32 v105, v105\n\t" \
    "v_exp_f32 v106, v106\n\t" \
    "v_exp_f32 v107, v107\n\t" \
    "v_exp_f32 v108, v108\n\t" \
    "v_exp_f32 v109, v109\n\t" \
    "s_waitcnt lgkmcnt(0)\n\t" \
    "v_smfmac_f32_32x32x32_bf16 v[60:75], v[144:147], v[100:107], v152\n\t" \
    "ds_read_b128 v[148:151], v155 offset:2080\n\t" \
    "ds_read_b128 v[132:135], v153 offset:6240\n\t" \
    "v_exp_f32 v110, v110\n\t" \
    "v_exp_f32 v111, v111\n\t" \
    "v_exp_f32 v112, v112\n\t" \
    "v_exp_f32 v113, v113\n\t" \
    "v_mfma_f32_32x32x16_bf16 v[92:107], v[128:131], v[124:127], v[76:91]\n\t" \
    "ds_read_b128 v[136:139], v154 offset:4160\n\t" \
    "v_exp_f32 v114, v114\n\t" \
    "v_exp_f32 v115, v115\n\t" \
    "v_exp_f32 v116, v116\n\t" \
    "v_exp_f32 v117, v117\n\t" \
    "v_exp_f32 v118, v118\n\t" \
    "v_exp_f32 v119, v119\n\t" \
    "v_smfmac_f32_32x32x32_bf16 v[60:75], v[140:143], v[108:115], v152\n\t" \
    "v_exp_f32 v120, v120\n\t" \
    "v_exp_f32 v121, v121\n\t" \
    "v_exp_f32 v122, v122\n\t" \
    "v_exp_f32 v123, v123\n\t" \
    "v_exp_f32 v92, v92\n\t" \
    "v_exp_f32 v93, v93\n\t" \
    "s_waitcnt lgkmcnt(0)\n\t" \
    "v_smfmac_f32_32x32x32_bf16 v[60:75], v[148:151], v[116:123], v152\n\t" \
    "ds_read_b128 v[144:147], v155 offset:4160\n\t" \
    "ds_read_b128 v[128:131], v153 offset:8320\n\t" \
    "v_exp_f32 v94, v94\n\t" \
    "v_exp_f32 v95, v95\n\t" \
    "v_exp_f32 v96, v96\n\t" \
    "v_exp_f32 v97, v97\n\t" \
    "v_mfma_f32_32x32x16_bf16 v[108:123], v[132:135], v[124:127], v[76:91]\n\t" \
    "ds_read_b128 v[140:143], v154 offset:6240\n\t" \
    "v_exp_f32 v98, v98\n\t" \
    "v_exp_f32 v99, v99\n\t" \
    "v_exp_f32 v100, v100\n\t" \
    "v_exp_f32 v101, v101\n\t" \
    "v_exp_f32 v102, v102\n\t" \
    "v_exp_f32 v103, v103\n\t" \
    "v_smfmac_f32_32x32x32_bf16 v[60:75], v[136:139], v[92:99], v152\n\t" \
    "v_exp_f32 v104, v104\n\t" \
    "v_exp_f32 v105, v105\n\t" \
    "v_exp_f32 v106, v106\n\t" \
    "v_exp_f32 v107, v107\n\t" \
    "v_exp_f32 v108, v108\n\t" \
    "v_exp_f32 v109, v109\n\t" \
    "s_waitcnt lgkmcnt(0)\n\t" \
    "v_smfmac_f32_32x32x32_bf16 v[60:75], v[144:147], v[100:107], v152\n\t" \
    "ds_read_b128 v[148:151], v155 offset:6240\n\t" \
    "ds_read_b128 v[132:135], v153 offset:10400\n\t" \
    "v_exp_f32 v110, v110\n\t" \
    "v_exp_f32 v111, v111\n\t" \
    "v_exp_f32 v112, v112\n\t" \
    "v_exp_f32 v113, v113\n\t" \
    "v_mfma_f32_32x32x16_bf16 v[92:107], v[128:131], v[124:127], v[76:91]\n\t" \
    "ds_read_b128 v[136:139], v154 offset:8320\n\t" \
    "v_exp_f32 v114, v114\n\t" \
    "v_exp_f32 v115, v115\n\t" \
    "v_exp_f32 v116, v116\n\t" \
    "v_exp_f32 v117, v117\n\t" \
    "v_exp_f32 v118, v118\n\t" \
    "v_exp_f32 v119, v119\n\t" \
    "v_smfmac_f32_32x32x32_bf16 v[60:75], v[140:143], v[108:115], v152\n\t" \
    "v_exp_f32 v120, v120\n\t" \
    "v_exp_f32 v121, v121\n\t" \
    "v_exp_f32 v122, v122\n\t" \
    "v_exp_f32 v123, v123\n\t" \
    "v_exp_f32 v92, v92\n\t" \
    "v_exp_f32 v93, v93\n\t" \
    "s_waitcnt lgkmcnt(0)\n\t" \
    "v_smfmac_f32_32x32x32_bf16 v[60:75], v[148:151], v[116:123], v152\n\t" \
    "ds_read_b128 v[144:147], v155 offset:8320\n\t" \
    "ds_read_b128 v[128:131], v153 offset:12480\n\t" \
    "v_exp_f32 v94, v94\n\t" \
    "v_exp_f32 v95, v95\n\t" \
    "v_exp_f32 v96, v96\n\t" \
    "v_exp_f32 v97, v97\n\t" \
    "v_mfma_f32_32x32x16_bf16 v[108:123], v[132:135], v[124:127], v[76:91]\n\t" \
    "ds_read_b128 v[140:143], v154 offset:10400\n\t" \
    "v_exp_f32 v98, v98\n\t" \
    "v_exp_f32 v99, v99\n\t" \
    "v_exp_f32 v100, v100\n\t" \
    "v_exp_f32 v101, v101\n\t" \
    "v_exp_f32 v102, v102\n\t" \
    "v_exp_f32 v103, v103\n\t" \
    "v_smfmac_f32_32x32x32_bf16 v[60:75], v[136:139], v[92:99], v152\n\t" \
    "v_exp_f32 v104, v104\n\t" \
    "v_exp_f32 v105, v105\n\t" \
    "v_exp_f32 v106, v106\n\t" \
    "v_exp_f32 v107, v107\n\t" \
    "v_exp_f32 v108, v108\n\t" \
    "v_exp_f32 v109, v109\n\t" \
    "s_waitcnt lgkmcnt(0)\n\t" \
    "v_smfmac_f32_32x32x32_bf16 v[60:75], v[144:147], v[100:107], v152\n\t" \
    "ds_read_b128 v[148:151], v155 offset:10400\n\t" \
    "ds_read_b128 v[132:135], v153 offset:14560\n\t" \
    "v_exp_f32 v110, v110\n\t" \
    "v_exp_f32 v111, v111\n\t" \
    "v_exp_f32 v112, v112\n\t" \
    "v_exp_f32 v113, v113\n\t" \
    "v_mfma_f32_32x32x16_bf16 v[92:107], v[128:131], v[124:127], v[76:91]\n\t" \
    "ds_read_b128 v[136:139], v154 offset:12480\n\t" \
    "v_exp_f32 v114, v114\n\t" \
    "v_exp_f32 v115, v115\n\t" \
    "v_exp_f32 v116, v116\n\t" \
    "v_exp_f32 v117, v117\n\t" \
    "v_exp_f32 v118, v118\n\t" \
    "v_exp_f32 v119, v119\n\t" \
    "v_smfmac_f32_32x32x32_bf16 v[60:75], v[140:143], v[108:115], v152\n\t" \
    "v_exp_f32 v120, v120\n\t" \
    "v_exp_f32 v121, v121\n\t" \
    "v_exp_f32 v122, v122\n\t" \
    "v_exp_f32 v123, v123\n\t" \
    "v_exp_f32 v92, v92\n\t" \
    "v_exp_f32 v93, v93\n\t" \
    "s_waitcnt lgkmcnt(0)\n\t" \
    "v_smfmac_f32_32x32x32_bf16 v[60:75], v[148:151], v[116:123], v152\n\t" \
    "ds_read_b128 v[144:147], v155 offset:12480\n\t" \
    "ds_read_b128 v[128:131], v153 offset:16640\n\t" \
    "v_exp_f32 v94, v94\n\t" \
    "v_exp_f32 v95, v95\n\t" \
    "v_exp_f32 v96, v96\n\t" \
    "v_exp_f32 v97, v97\n\t" \
    "v_mfma_f32_32x32x16_bf16 v[108:123], v[132:135], v[124:127], v[76:91]\n\t" \
    "ds_read_b128 v[140:143], v154 offset:14560\n\t" \
    "v_exp_f32 v98, v98\n\t" \
    "v_exp_f32 v99, v99\n\t" \
    "v_exp_f32 v100, v100\n\t" \
    "v_exp_f32 v101, v101\n\t" \
    "v_exp_f32 v102, v102\n\t" \
    "v_exp_f32 v103, v103\n\t" \
    "v_smfmac_f32_32x32x32_bf16 v[60:75], v[136:139], v[92:99], v152\n\t" \
    "v_exp_f32 v104, v104\n\t" \
    "v_exp_f32 v105, v105\n\t" \
    "v_exp_f32 v106, v106\n\t" \
    "v_exp_f32 v107, v107\n\t" \
    "v_exp_f32 v108, v108\n\t" \
    "v_exp_f32 v109, v109\n\t" \
    "s_waitcnt lgkmcnt(0)\n\t" \
    "v_smfmac_f32_32x32x32_bf16 v[60:75], v[144:147], v[100:107], v152\n\t" \
    "ds_read_b128 v[148:151], v155 offset:14560\n\t" \
    "ds_read_b128 v[132:135], v153 offset:18720\n\t" \
    "v_exp_f32 v110, v110\n\t" \
    "v_exp_f32 v111, v111\n\t" \
    "v_exp_f32 v112, v112\n\t" \
    "v_exp_f32 v113, v113\n\t" \
    "v_mfma_f32_32x32x16_bf16 v[92:107], v[128:131], v[124:127], v[76:91]\n\t" \
    "ds_read_b128 v[136:139], v154 offset:16640\n\t" \
    "v_exp_f32 v114, v114\n\t" \
    "v_exp_f32 v115, v115\n\t" \
    "v_exp_f32 v116, v116\n\t" \
    "v_exp_f32 v117, v117\n\t" \
    "v_exp_f32 v118, v118\n\t" \
    "v_exp_f32 v119, v119\n\t" \
    "v_smfmac_f32_32x32x32_bf16 v[60:75], v[140:143], v[108:115], v152\n\t" \
    "v_exp_f32 v120, v120\n\t" \
    "v_exp_f32 v121, v121\n\t" \
    "v_exp_f32 v122, v122\n\t" \
    "v_exp_f32 v123, v123\n\t" \
    "v_add_u32 v153, 16640, v153\n\t" \
    "v_add_u32 v154, 16640, v154\n\t" \
    "v_add_u32 v155, 16640, v155\n\t" \
    "s_sub_u32 %[cnt], %[cnt], 1\n\t" \
    "s_cmp_lg_u32 %[cnt], %[sig]\n\t" \
    "s_cbranch_scc1 5f\n\t" \
    "s_waitcnt vmcnt(0)\n\t" \
    "v_mov_b32 v156, %[ca]\n\t" \
    "v_mov_b32 v157, 1\n\t" \
    "s_mov_b64 exec, 1\n\t" \
    "ds_add_u32 v156, v157 offset:36\n\t" \
    "s_mov_b64 exec, -1\n\t" \
    "5:\n\t" \
    "s_cmp_lg_u32 %[cnt], %[k1]\n\t" \
    "s_cbranch_scc1 2f\n\t" \
    "v_mov_b32 v157, %[ca]\n\t" \
    "6:\n\t" \
    "ds_read_b32 v156, v157 offset:40\n\t" \
    "s_waitcnt lgkmcnt(0)\n\t" \
    "v_readfirstlane_b32 %[st], v156\n\t" \
    "s_nop 3\n\t" \
    "s_cmp_lt_u32 %[st], 4\n\t" \
    "s_cbranch_scc1 6b\n\t" \
    "2:\n\t" \
    "s_cmp_lg_u32 %[cnt], %[k2]\n\t" \
    "s_cbranch_scc1 8f\n\t" \
    "v_mov_b32 v157, %[ca]\n\t" \
    "7:\n\t" \
    "ds_read_b32 v156, v157 offset:0\n\t" \
    "s_waitcnt lgkmcnt(0)\n\t" \
    "v_readfirstlane_b32 %[st], v156\n\t" \
    "s_nop 3\n\t" \
    "s_cmp_lt_u32 %[st], 4\n\t" \
    "s_cbranch_scc1 7b\n\t" \
    "8:\n\t" \
    "s_cmp_lg_u32 %[cnt], 5\n\t" \
    "s_cbranch_scc1 3f\n\t" \
    "s_setprio 1\n\t" \
    "3:\n\t" \
    "s_cmp_lg_u32 %[cnt], 2\n\t" \
    "s_cbranch_scc1 4f\n\t" \
    "s_setprio 0\n\t" \
    "4:\n\t" \
    "s_cmp_lg_u32 %[cnt], 0\n\t" \
    "s_cbranch_scc1 1b\n\t" \
    "s_waitcnt lgkmcnt(0)\n\t" \
    "s_nop 1\n\t" \
    "v_smfmac_f32_32x32x32_bf16 v[60:75], v[148:151], v[116:123], v152\n\t" \
    "s_nop 15\n\t" \
    "v_mov_b32 %[o0], v60\n\t" \
    "v_mov_b32 %[o1], v61\n\t" \
    "v_mov_b32 %[o2], v62\n\t" \
    "v_mov_b32 %[o3], v63\n\t" \
    "v_mov_b32 %[o4], v64\n\t" \
    "v_mov_b32 %[o5], v65\n\t" \
    "v_mov_b32 %[o6], v66\n\t" \
    "v_mov_b32 %[o7], v67\n\t" \
    "v_mov_b32 %[o8], v68\n\t" \
    : [o0] "=&v"(o[0]), [o1] "=&v"(o[1]), [o2] "=&v"(o[2]), [o3] "=&v"(o[3]), [o4] "=&v"(o[4]), [o5] "=&v"(o[5]), [o6] "=&v"(o[6]), [o7] "=&v"(o[7]), [o8] "=&v"(o[8]), [cnt] "=&s"(cnt_), [st] "=&s"(st_) \
    : [q0] "v"(q[0]), [q1] "v"(q[1]), [q2] "v"(q[2]), [q3] "v"(q[3]), [nm] "v"(nm_), [ka] "v"(ka_), [va] "v"(va_), [vb] "v"(vb_), [k1] "s"(k1_), [k2] "s"(k2_), [sig] "s"(sig_), [ca] "s"(ca_) \
    : "memory", "scc", "v60", "v61", "v62", "v63", "v64", "v65", "v66", "v67", "v68", "v69", "v70", "v71", "v72", "v73", "v74", "v75", "v76", "v77", "v78", "v79", "v80", "v81", "v82", "v83", "v84", "v85", "v86", "v87", "v88", "v89", "v90", "v91", "v92", "v93", "v94", "v95", "v96", "v97", "v98", "v99", "v100", "v101", "v102", "v103", "v104", "v105", "v106", "v107", "v108", "v109", "v110", "v111", "v112", "v113", "v114", "v115", "v116", "v117", "v118", "v119", "v120", "v121", "v122", "v123", "v124", "v125", "v126", "v127", "v128", "v129", "v130", "v131", "v132", "v133", "v134", "v135", "v136", "v137", "v138", "v139", "v140", "v141", "v142", "v143", "v144", "v145", "v146", "v147", "v148", "v149", "v150", "v151", "v152", "v153", "v154", "v155", "v156", "v157"); } while (0)


struct Params {
    const unsigned short* x; const unsigned short* wq; const unsigned short* wk; const unsigned short* wv; const unsigned short* wo;
    const unsigned short* w1; const unsigned short* w2; unsigned short* out; char* ws;
};

__global__ void __launch_bounds__(768, 3) fused_block_kernel(Params P) {
    __shared__ __attribute__((aligned(16))) char lds_raw[LDS_BYTES];
    LAS char* lds = (LAS char*)lds_raw;
    const unsigned lds0 = (unsigned)(uintptr_t)lds;
    const int tid = threadIdx.x, lane = tid & 63;
    const int w = __builtin_amdgcn_readfirstlane(tid >> 6);
    const int b = blockIdx.x;
    const int p = ((b & 7) >> 1) * 8 + ((b >> 3) & 7);
    const int c = (b & 1) * 4 + (b >> 6);
    const int kvkind = c == (p & 7) ? 0 : (c == ((p + 4) & 7) ? 1 : -1);
    const int r32 = lane & 31, h = lane >> 5;
    char* ws = P.ws;
    const __amdgpu_buffer_rsrc_t wsr = __builtin_amdgcn_make_buffer_rsrc(ws, 0, WS_END, 0x00020000);
    const int su = w & 3;
    const int ft0 = c < 3 ? 6 * c : 18 + 5 * (c - 3);
    const int nft = c < 3 ? 6 : 5;
    u64 epoch;
    unsigned warm;

    const __amdgpu_buffer_rsrc_t wr = su == 0 ? __builtin_amdgcn_make_buffer_rsrc((void*)P.wq, 0, DM * DM * 2, 0x00020000)
                                              : __builtin_amdgcn_make_buffer_rsrc((void*)P.w1, 0, DM * FF * 2, 0x00020000);
    const unsigned wstride = su == 0 ? 1024u : (unsigned)(FF * 2);
    const unsigned wvoff = (unsigned)(lane >> 3) * wstride + (su == 0 ? c * 128 : ft0 * 64 + (su - 1) * 128) + (((lane & 7) ^ (((lane >> 4) & 1) << 2)) << 4);
    auto stage = [&](int j) {
#pragma unroll
        for (int i = 0; i < 4; ++i) dma16(wr, wvoff, (unsigned)(32 * j + 8 * i) * wstride, lds0 + L_RING + ((j + 4) % 5) * 16384 + su * 4096 + i * 1024);
    };
    unsigned rl[2][8];
    if (w >= 8) {
        if (kvkind >= 0) {
            const __amdgpu_buffer_rsrc_t kr = __builtin_amdgcn_make_buffer_rsrc((void*)(kvkind == 0 ? P.wk : P.wv), 0, DM * 16 * 2, 0x00020000);
#pragma unroll
            for (int i = 0; i < 4; ++i) dma16(kr, (lane * 16) ^ (((lane >> 4) & 1) << 7), (su * 4 + i) * 1024, lds0 + L_E + (su * 4 + i) * 1024);
        }
#pragma unroll
        for (int j = 0; j < 5; ++j) stage(j);
        epoch = __hip_atomic_load((gu64*)(ws + WS_E), RLX_AGENT);
#pragma unroll
        for (int k = 0; k < 2; ++k) {
            const int pc = su + 4 * k, idx = pc >> 1, nt = pc & 1;
            const int s = idx < 3 ? p + 32 * idx : p;
            const unsigned short* src = (idx < 3 ? P.w2 : P.wo) + (size_t)(16 * (s < NKS2 ? s : 0) + 4 * h) * DM + 64 * c + 32 * nt + r32;
            asm volatile("global_load_ushort %0, %8, off\n\tglobal_load_ushort %1, %8, off offset:1024\n\t"
                         "global_load_ushort %2, %8, off offset:2048\n\tglobal_load_ushort %3, %8, off offset:3072\n\t"
                         "global_load_ushort %4, %9, off\n\tglobal_load_ushort %5, %9, off offset:1024\n\t"
                         "global_load_ushort %6, %9, off offset:2048\n\tglobal_load_ushort %7, %9, off offset:3072"
                         : "=&v"(rl[k][0]), "=&v"(rl[k][1]), "=&v"(rl[k][2]), "=&v"(rl[k][3]), "=&v"(rl[k][4]), "=&v"(rl[k][5]), "=&v"(rl[k][6]), "=&v"(rl[k][7])
                         : "v"(src), "v"(src + 8 * DM) : "memory");
        }
        WAIT_VM(25);
    } else {
        const int tk = 8 * w + (lane >> 3), part = lane & 7;
        const u32x4* xr = (const u32x4*)((const char*)P.x + (size_t)(64 * p + tk) * 1024) + part;
        u32x4 xv[8];
#pragma unroll
        for (int i = 0; i < 8; ++i) xv[i] = xr[8 * i];
        epoch = __hip_atomic_load((gu64*)(ws + WS_E), RLX_AGENT);
        warm = 0;
        if (w >= 4) {
            const int k = (w - 4) * 64 + lane, row = 160 + 44 * (p & 7) + k / 5, ln = k % 5;
            if (k < 220) warm = ln == 0 ? *(const GAS unsigned*)((const char*)P.wq + (size_t)row * 1024 + c * 128)
                                        : *(const GAS unsigned*)((const char*)P.w1 + (size_t)row * (FF * 2) + min(ft0 * 64 + (ln - 1) * 128, FF * 2 - 4));
        }
        f32x2 f[32];
        f32x2 s2 = {0.f, 0.f}, q2 = {0.f, 0.f}, s3 = s2, q3 = s2;
#pragma unroll
        for (int i = 0; i < 8; ++i)
#pragma unroll
            for (int j = 0; j < 4; ++j) f[4 * i + j] = (f32x2){bf_lo(xv[i][j]), bf_hi(xv[i][j])};
#pragma unroll
        for (int i = 0; i < 32; i += 2) { s2 += f[i]; q2 += f[i] * f[i]; s3 += f[i + 1]; q3 += f[i + 1] * f[i + 1]; }
        float s = (s2[0] + s2[1]) + (s3[0] + s3[1]), q = (q2[0] + q2[1]) + (q3[0] + q3[1]);
        s = red8(s); q = red8(q);
        const float mean = s * (1.0f / DM);
        const float var = fmaxf(q * (1.0f / DM) - mean * mean, 0.f);
        const float rstd = __builtin_amdgcn_rsqf(var + 1e-5f);
        const f32x2 r2 = {rstd, rstd}, m2 = {-mean * rstd, -mean * rstd};
        LAS char* xd = lds + L_XN + (tk >> 5) * 32768 + part * 512 + (((tk & 31) ^ part) << 4);
#pragma unroll
        for (int i = 0; i < 8; ++i) {
            u32x4 o;
#pragma unroll
            for (int j = 0; j < 4; ++j) { const f32x2 y = f[4 * i + j] * r2 + m2; o[j] = cvt_pk(y[0], y[1]); }
            *(LAS u32x4*)(xd + i * 4096) = o;
        }

    }
#define tag (epoch + 1)
    BARRIER();
    f32x16 acc[2][2];
    if (w < 4) {
#pragma unroll
        for (int i = 0; i < 2; ++i)
#pragma unroll
            for (int j = 0; j < 2; ++j) acc[i][j] = (f32x16){};
        const int G = lane >> 4, i16 = lane & 15, q4 = i16 >> 2, swz = (q4 >> 1) & 1;
        const int abase = L_RING + w * 4096 + (8 * (G >> 1) + q4) * 128 + (G & 1) * 32 + (i16 & 3) * 8;
        const LAS char* a0 = lds + abase + swz * 64;
        const LAS char* a1 = lds + abase + (swz ^ 1) * 64;
        const LAS char* bb[4];
#pragma unroll
        for (int s = 0; s < 4; ++s) bb[s] = lds + L_XN + (2 * s + h) * 512 + ((r32 ^ (2 * s + h)) << 4);
        bf16x8 fa0[2], fa1[2], fb0[2], fb1[2];
        auto so = [&](int ks) { return (((ks >> 1) + 4) % 5) * 16384 + (ks & 1) * 2048; };
        auto bo = [&](int ks) { return bb[ks & 3] + (ks >> 2) * 4096; };
        fa0[0] = tr_frag(a0 + so(0), a0 + so(0) + 512); fb0[0] = lds_ld16(bo(0));
        fa1[0] = tr_frag(a1 + so(0), a1 + so(0) + 512); fb1[0] = lds_ld16(bo(0) + 32768);
#pragma unroll
        for (int ks = 0; ks < 32; ++ks) {
            const int cb = ks & 1, nb = cb ^ 1, kn = ks + 1;
            acc[0][0] = MFMA32(fa0[cb], fb0[cb], acc[0][0]);
            SB();
            if ((ks & 1) && ks >= 5) BARRIER();
            if (ks == 2 * KVJ + 1 && w == 0 && kvkind >= 0 && lane < 8)
                __hip_atomic_store((gu64*)(ws + WS_FKV + (lane * 64 + 2 * p + kvkind) * 128), tag, RLX_AGENT);
            if (kn < 32) { fa0[nb] = tr_frag(a0 + so(kn), a0 + so(kn) + 512); fb0[nb] = lds_ld16(bo(kn)); }
            SB();
            acc[1][0] = MFMA32(fa1[cb], fb0[cb], acc[1][0]);
            SB();
            if (kn < 32) fa1[nb] = tr_frag(a1 + so(kn), a1 + so(kn) + 512);
            SB();
            acc[0][1] = MFMA32(fa0[cb], fb1[cb], acc[0][1]);
            SB();
            if (kn < 32) fb1[nb] = lds_ld16(bo(kn) + 32768);
            SB();
            acc[1][1] = MFMA32(fa1[cb], fb1[cb], acc[1][1]);
            SB();
        }
    } else if (w < 8) {
        u64 seen = 0;
        if (kvkind >= 0) {
            __builtin_amdgcn_s_setprio(3);
            const int g = lane >> 4, i16 = lane & 15;
            const LAS char* wp = lds + L_E + (((8 * g + (i16 >> 2)) * 32 + (i16 & 3) * 8) ^ ((g & 1) << 7));
            const LAS char* wq2 = lds + L_E + (((8 * g + 4 + (i16 >> 2)) * 32 + (i16 & 3) * 8) ^ ((g & 1) << 7));
            const int tr = (su & 1) * 16 + i16;
            const LAS char* xp0 = lds + L_XN + (su >> 1) * 32768 + g * 512 + ((tr ^ g) << 4);
            const LAS char* xp1 = lds + L_XN + (su >> 1) * 32768 + (4 + g) * 512 + ((tr ^ (4 + g)) << 4);
            f32x4 k0 = {0.f, 0.f, 0.f, 0.f}, k1 = k0;
#pragma unroll
            for (int hh = 0; hh < 2; ++hh) {
                bf16x8 wf[8], xf[8];
#pragma unroll
                for (int s = 0; s < 8; ++s) {
                    const int ss = 8 * hh + s;
                    wf[s] = tr_frag(wp + ss * 1024, wq2 + ss * 1024);
                    xf[s] = lds_ld16(((s & 1) ? xp1 : xp0) + (ss >> 1) * 4096);
                }
                if (kvkind == 0) {
#pragma unroll
                    for (int s = 0; s < 8; s += 2) { k0 = MFMA16(wf[s], xf[s], k0); k1 = MFMA16(wf[s + 1], xf[s + 1], k1); }
                } else {
#pragma unroll
                    for (int s = 0; s < 8; s += 2) { k0 = MFMA16(xf[s], wf[s], k0); k1 = MFMA16(xf[s + 1], wf[s + 1], k1); }
                }
            }
            k0 += k1;
            const int t = 2 * p + (su >> 1);
            const unsigned x0 = cvt_pk(k0[0], k0[1]), x1 = cvt_pk(k0[2], k0[3]);
            if (kvkind == 0) {
                auto r0 = __builtin_amdgcn_permlane32_swap(x0, x0, false, false), r1 = __builtin_amdgcn_permlane32_swap(x1, x1, false, false);
                if (lane < 32) __builtin_amdgcn_raw_buffer_store_b128((u32x4){x0, x1, r0[1], r1[1]}, wsr, WS_KF + t * 1024 + g * 512 + tr * 16, 0, SC1);
            } else {
                auto r0 = __builtin_amdgcn_permlane16_swap(x0, x0, false, false), r1 = __builtin_amdgcn_permlane16_swap(x1, x1, false, false);
                if (!(g & 1)) __builtin_amdgcn_raw_buffer_store_b128((u32x4){x0, x1, r0[1], r1[1]}, wsr, WS_VF + t * 1024 + (su & 1) * 512 + (g >> 1) * 256 + i16 * 16, 0, SC1);
            }
            __builtin_amdgcn_s_setprio(0);
        }
        const GAS char* fkv = (const GAS char*)ws + WS_FKV + ((b & 7) * 64 + lane) * 128;
        auto kv_tiles = [&](int t0, int n) {
#pragma unroll
            for (int i = 0; i < n / 2; ++i) {
                const int it = su + 4 * i, t = t0 + (it >> 1);
                dma16_sc1(wsr, lane * 16, ((it & 1) ? WS_VF : WS_KF) + t * 1024, lds0 + t * TS + (it & 1) * 1024);
            }
        };
        bool early = false;
#pragma unroll
        for (int j = 2; j < 16; ++j) {
            if (j == KVJ && kvkind >= 0) WAIT_VM(0);
            if (j == POLLJ)
                asm volatile("global_load_dwordx2 %0, %1, off sc1" : "=v"(seen) : "v"(fkv) : "memory");
            if (j == CHKJ) {
                asm volatile("s_waitcnt vmcnt(0)" : "+v"(seen) :: "memory");
                asm volatile("" :: "v"(warm));
                early = __all(seen == tag);
                if (early) { kv_tiles(0, 14); kv_tiles(14, 8); kv_tiles(22, 8); }
            }
            BARRIER();
        }
        if (!early) { wait_flags((gu64*)fkv, tag, true); kv_tiles(0, 14); kv_tiles(14, 8); kv_tiles(22, 8); }
        kv_tiles(30, 8);
    } else {
#pragma unroll
        for (int j = 2; j < 16; ++j) {
            if (j == 2) WAIT_VM(21); else if (j == 3) WAIT_VM(29); else if (j <= 11) WAIT_VM(12); else if (j == 12) WAIT_VM(8); else if (j == 13) WAIT_VM(4); else if (j == 14) WAIT_VM(0);
            BARRIER();
            if (j == 2) { stage(5); stage(6); }
            if (j + 5 < 16) stage(j + 5);
        }
    }
    if (w == 0) {
#pragma unroll
        for (int hl = 0; hl < 4; ++hl)
#pragma unroll
            for (int th = 0; th < 2; ++th) {
                const f32x16& a = acc[hl >> 1][th];
                const int o8 = (hl & 1) * 8;
                u32x4 o;
#pragma unroll
                for (int j = 0; j < 4; ++j) o[j] = cvt_pk(a[o8 + 2 * j] * QSCALE, a[o8 + 2 * j + 1] * QSCALE);
                *(LAS u32x4*)(lds + L_QF + (hl * 2 + th) * 1024 + lane * 16) = o;
            }
    } else if (w < 4) {
#pragma unroll
        for (int ft = 0; ft < 2; ++ft) {
            const int lt = 2 * (w - 1) + ft;
            if (lt < nft) {
#pragma unroll
                for (int th = 0; th < 2; ++th)
#pragma unroll
                    for (int s = 0; s < 2; ++s) {
                        const f32x16& a = acc[ft][th];
                        u32x4 o;
#pragma unroll
                        for (int j = 0; j < 4; ++j) o[j] = cvt_pk(fmaxf(a[8 * s + 2 * j], 0.f), fmaxf(a[8 * s + 2 * j + 1], 0.f));
                        __builtin_amdgcn_raw_buffer_store_b128(o, wsr, WS_HF + ((p * 2 + th) * NKS2 + 2 * (ft0 + lt) + s) * 1024 + lane * 16, 0, SC1);
                    }
            }
        }
    }
    BARRIER();
    if (tid < 128) *(LAS u32x4*)(lds + (tid >> 1) * TS + 2048 + (tid & 1) * 16) = (tid & 1) ? (u32x4){0u, 0u, 0u, 0u} : (u32x4){0x3F803F80u, 0x3F803F80u, 0x3F803F80u, 0x3F803F80u};
    if (tid >= 128 && tid < 144) *(LAS unsigned*)(lds + L_CNT + (tid - 128) * 4) = 0u;
    if (w >= 4 && w < 8) WAIT_VM(12);
    BARRIER();
    if (w < 8) {
        if (w >= 4) {
            WAIT_VM(0);
            if (lane == 0) __hip_atomic_fetch_add((LAS unsigned*)(lds + L_CNT + 40), 1u, __ATOMIC_RELAXED, __HIP_MEMORY_SCOPE_WORKGROUP);
        }
        const int ath = w & 1;
        const u32x4 qf = *(const LAS u32x4*)(lds + L_QF + w * 1024 + lane * 16);
        const LAS char* kp = lds + lane * 16;
        const int co = r32 == 16 ? 2048 : 2064;
        const unsigned ka = lds0 + lane * 16;
        const unsigned vaa = lds0 + (r32 < 16 ? 1024 + h * 256 + r32 * 16 : co), vba = lds0 + (r32 < 16 ? 1536 + h * 256 + r32 * 16 : co);
        float oa[9];
        for (int attempt = 0; attempt < 2; ++attempt) {
            const int nmax = attempt == 0 ? 2 : 64;
            float mx = -3.0e38f;
            for (int t = 0; t < nmax; ++t) {
                const f32x16 z = {};
                const f32x16 sa = MFMA32(lds_ld16(kp + t * TS), __builtin_bit_cast(bf16x8, qf), z);
#pragma unroll
                for (int j = 0; j < 16; j += 2) mx = fmaxf(mx, fmaxf(sa[j], sa[j + 1]));
            }
            mx = fmaxf(mx, __shfl_xor(mx, 32));
            const float nm = -(mx + (attempt == 0 ? MBIAS : 0.f));
            const unsigned none = 0xffffffffu, k1 = attempt == 0 ? 7u : none, k2 = attempt == 0 ? 4u : none, sig = (attempt == 0 && w < 4) ? (unsigned)SIGC : none;
            ATT_LOOP(oa, qf, nm, ka, vaa, vba, k1, k2, sig, lds0 + L_CNT);
            const float l = __shfl(oa[8], r32);
            oa[8] = l;
            bool ok = (l > 1.0e-30f) && (l < 1.0e37f);
#pragma unroll
            for (int j = 0; j < 8; ++j) ok = ok && (fabsf(oa[j]) < 3.0e38f);
            if (__all(ok)) break;
        }
        const int tth = w & 1, kq = w >> 1;
        const unsigned lo = lane * 16;
        bf16x8 ga0[8], ga1[8], gb[8];
        for (unsigned sp = 0; sp < (1u << 24); ++sp) { if (__hip_atomic_load((LAS unsigned*)(lds + L_CNT + 32), __ATOMIC_RELAXED, __HIP_MEMORY_SCOPE_WORKGROUP)) break; __builtin_amdgcn_s_sleep(1); }
        const unsigned wo0 = WS_WOF + ((c * 2 + 0) * 32 + 8 * kq) * 1024, wo1 = wo0 + 32 * 1024;
#pragma unroll
        for (int i = 0; i < 8; ++i) { ga0[i] = ld_frag(wsr, lo, wo0 + i * 1024); ga1[i] = ld_frag(wsr, lo, wo1 + i * 1024); }
        const float inv = 1.0f / oa[8];
        u32x4 o0;
#pragma unroll
        for (int j = 0; j < 4; ++j) o0[j] = cvt_pk(oa[2 * j] * inv, oa[2 * j + 1] * inv);
        __builtin_amdgcn_raw_buffer_store_b128(o0, wsr, WS_OF + ((p * 2 + ath) * 32 + 4 * c + (w >> 1)) * 1024 + lo, 0, SC1);
        WAIT_VM(0);
        if (lane == 0) __hip_atomic_store((gu64*)(ws + WS_FO + (b * 8 + w) * 128), tag, RLX_AGENT);
        BARRIER();
        {
            const int cc = 2 * kq + ((lane >> 2) & 1), ww = (lane & 3) * 2 + tth;
            const int bb = block_of(p, cc);
            wait_flags((gu64*)(ws + WS_FO + (bb * 8 + ww) * 128), tag, lane < 8);
        }
        const unsigned oo = WS_OF + ((p * 2 + tth) * 32 + 8 * kq) * 1024;
#pragma unroll
        for (int i = 0; i < 8; ++i) gb[i] = ld_frag(wsr, lo, oo + i * 1024);
        f32x16 y0 = {}, y1 = {};
#pragma unroll
        for (int i = 0; i < 8; ++i) { y0 = MFMA32(ga0[i], gb[i], y0); y1 = MFMA32(ga1[i], gb[i], y1); }
        const int fnt = kq >> 1, fq = 2 * (kq & 1);
        LAS char* mine = lds + w * 8192 + lane * 16;
#pragma unroll
        for (int q = 0; q < 4; ++q) {
            if (!(fnt == 0 && (q >> 1) == (kq & 1))) *(LAS f32x4*)(mine + q * 1024) = (f32x4){y0[4 * q], y0[4 * q + 1], y0[4 * q + 2], y0[4 * q + 3]};
            if (!(fnt == 1 && (q >> 1) == (kq & 1))) *(LAS f32x4*)(mine + 4096 + q * 1024) = (f32x4){y1[4 * q], y1[4 * q + 1], y1[4 * q + 2], y1[4 * q + 3]};
        }
        BARRIER();
        f32x4 z[2];
#pragma unroll
        for (int q = 0; q < 2; ++q) {
            const int qq = fq + q;
            z[q] = fnt == 0 ? (kq & 1 ? (f32x4){y0[8 + 4 * q], y0[9 + 4 * q], y0[10 + 4 * q], y0[11 + 4 * q]} : (f32x4){y0[4 * q], y0[4 * q + 1], y0[4 * q + 2], y0[4 * q + 3]})
                            : (kq & 1 ? (f32x4){y1[8 + 4 * q], y1[9 + 4 * q], y1[10 + 4 * q], y1[11 + 4 * q]} : (f32x4){y1[4 * q], y1[4 * q + 1], y1[4 * q + 2], y1[4 * q + 3]});
#pragma unroll
            for (int k = 1; k < 4; ++k) z[q] += *(const LAS f32x4*)(lds + (2 * (kq ^ k) + tth) * 8192 + fnt * 4096 + qq * 1024 + lane * 16);
            const int gi = ((fnt * 2 + tth) ^ 2);
            z[q] += *(const LAS f32x4*)(lds + (gi < 3 ? L_X0 + gi * 4096 : L_X3) + qq * 1024 + lane * 16);
        }
        const unsigned a0 = cvt_pk(z[0][0], z[0][1]), a1 = cvt_pk(z[0][2], z[0][3]), b0 = cvt_pk(z[1][0], z[1][1]), b1 = cvt_pk(z[1][2], z[1][3]);
        auto r0 = __builtin_amdgcn_permlane32_swap(a0, b0, false, false);
        auto r1 = __builtin_amdgcn_permlane32_swap(a1, b1, false, false);
        *(u32x4*)(P.out + (size_t)(64 * p + 32 * tth + r32) * DM + 64 * c + 32 * fnt + 8 * fq + 8 * h) = (u32x4){r0[0], r1[0], r0[1], r1[1]};
    } else {
        __builtin_amdgcn_s_setprio(3);
#pragma unroll
        for (int i = 0; i < 13; ++i) {
            const int it = su + 4 * i, t = NTA + (it >> 1);
            dma16_sc1(wsr, lane * 16, ((it & 1) ? WS_VF : WS_KF) + t * 1024, lds0 + t * TS + (it & 1) * 1024);
        }
        {
#pragma unroll
            for (int k = 0; k < 2; ++k) {
                const int pc = su + 4 * k, idx = pc >> 1, nt = pc & 1;
                const int s = idx < 3 ? p + 32 * idx : p;
                asm volatile("" : "+v"(rl[k][0]), "+v"(rl[k][1]), "+v"(rl[k][2]), "+v"(rl[k][3]), "+v"(rl[k][4]), "+v"(rl[k][5]), "+v"(rl[k][6]), "+v"(rl[k][7]));
                u32x4 o;
#pragma unroll
                for (int j = 0; j < 4; ++j) o[j] = rl[k][2 * j] | (rl[k][2 * j + 1] << 16);
                const unsigned off = (idx < 3 ? WS_W2F + ((c * 2 + nt) * NKS2 + s) * 1024 : WS_WOF + ((c * 2 + nt) * 32 + s) * 1024) + lane * 16;
                if (idx == 3 || s < NKS2) __builtin_amdgcn_raw_buffer_store_b128(o, wsr, off, 0, SC1);
            }
            WAIT_VM(0);
            unsigned old = 0;
            if (lane == 0) old = __hip_atomic_fetch_add((LAS unsigned*)(lds + L_CNT), 1u, __ATOMIC_RELAXED, __HIP_MEMORY_SCOPE_WORKGROUP);
            if (__builtin_amdgcn_readfirstlane(old) == 3) {
                for (unsigned sp = 0; sp < (1u << 24); ++sp) { if (__hip_atomic_load((LAS unsigned*)(lds + L_CNT + 36), __ATOMIC_RELAXED, __HIP_MEMORY_SCOPE_WORKGROUP) == 4u) break; __builtin_amdgcn_s_sleep(1); }
                if (lane == 0) __hip_atomic_store((gu64*)(ws + WS_FS1 + b * 128), tag, RLX_AGENT);
            }
        }
        if (b == 0 && w == 8) {
            wait_flags4((gu64*)(ws + WS_FS1 + lane * 128), tag);
        } else {
            const int pp = lane < 32 ? lane : p, cc = lane < 32 ? c : (lane & 7);
            const int bb = block_of(pp, cc);
            wait_flags((gu64*)(ws + WS_FS1 + bb * 128), tag, lane < 40);
        }
        if (lane == 0) __hip_atomic_store((LAS unsigned*)(lds + L_CNT + 32), 1u, __ATOMIC_RELAXED, __HIP_MEMORY_SCOPE_WORKGROUP);
        const int tth = su & 1, kh = su >> 1;
        f32x16 y0 = {}, y1 = {};
        const unsigned lo = lane * 16;
        const size_t orow = (size_t)(64 * p + 32 * tth + r32) * DM + 64 * c + 32 * kh;
        u32x2 xr[4];
#pragma unroll
        for (int q = 0; q < 4; ++q) xr[q] = *(const u32x2*)(P.x + orow + 8 * q + 4 * h);
        {
            const unsigned ao0 = WS_W2F + ((c * 2 + 0) * NKS2 + 43 * kh) * 1024, ao1 = ao0 + NKS2 * 1024;
            const unsigned bo = WS_HF + ((p * 2 + tth) * NKS2 + 43 * kh) * 1024;
            bf16x8 fa0[RING], fa1[RING], fb[RING];
#pragma unroll
            for (int i = 0; i < RING; ++i) { fa0[i] = ld_frag(wsr, lo, ao0 + i * 1024); fa1[i] = ld_frag(wsr, lo, ao1 + i * 1024); fb[i] = ld_frag(wsr, lo, bo + i * 1024); }
            SB();
#pragma unroll
            for (int s = 0; s < NFFN; ++s) {
                const int i = s % RING;
                y0 = MFMA32(fa0[i], fb[i], y0); y1 = MFMA32(fa1[i], fb[i], y1);
                SB();
                if (s + RING < NFFN) { fa0[i] = ld_frag(wsr, lo, ao0 + (s + RING) * 1024); fa1[i] = ld_frag(wsr, lo, ao1 + (s + RING) * 1024); fb[i] = ld_frag(wsr, lo, bo + (s + RING) * 1024); }
                SB();
            }
        }
        auto xbuf = [&](int k) { return lds + (k < 3 ? L_X0 + k * 4096 : L_X3) + lane * 16; };
        {
            LAS char* dst = xbuf(su);
#pragma unroll
            for (int q = 0; q < 4; ++q)
                *(LAS f32x4*)(dst + q * 1024) = kh == 0 ? (f32x4){y1[4 * q], y1[4 * q + 1], y1[4 * q + 2], y1[4 * q + 3]}
                                                        : (f32x4){y0[4 * q], y0[4 * q + 1], y0[4 * q + 2], y0[4 * q + 3]};
            asm volatile("s_waitcnt lgkmcnt(0)" ::: "memory");
            if (lane == 0) __hip_atomic_store((LAS unsigned*)(lds + L_CNT + 4 + su * 4), 1u, __ATOMIC_RELAXED, __HIP_MEMORY_SCOPE_WORKGROUP);
            for (unsigned sp = 0; sp < (1u << 24); ++sp) { if (__hip_atomic_load((LAS unsigned*)(lds + L_CNT + 4 + (su ^ 2) * 4), __ATOMIC_RELAXED, __HIP_MEMORY_SCOPE_WORKGROUP)) break; __builtin_amdgcn_s_sleep(1); }
            asm volatile("" ::: "memory");
            LAS char* src = xbuf(su ^ 2);
#pragma unroll
            for (int q = 0; q < 4; ++q) {
                f32x4 z = *(const LAS f32x4*)(src + q * 1024);
#pragma unroll
                for (int j = 0; j < 4; ++j) z[j] += kh == 0 ? y0[4 * q + j] : y1[4 * q + j];
                z[0] += bf_lo(xr[q][0]); z[1] += bf_hi(xr[q][0]); z[2] += bf_lo(xr[q][1]); z[3] += bf_hi(xr[q][1]);
                *(LAS f32x4*)(src + q * 1024) = z;
            }
        }
        BARRIER();
        BARRIER();
        if (b == 0 && w == 8 && lane == 0) __hip_atomic_store((gu64*)(ws + WS_E), tag, RLX_AGENT);
    }
}

extern "C" void kernel_launch(void* const* d_in, const int* in_sizes, int n_in, void* d_out, int out_size,
                              void* d_ws, size_t ws_size, hipStream_t stream) {
    Params P;
    P.x = (const unsigned short*)d_in[0]; P.wq = (const unsigned short*)d_in[1]; P.wk = (const unsigned short*)d_in[2];
    P.wv = (const unsigned short*)d_in[3]; P.wo = (const unsigned short*)d_in[4];
    P.w1 = (const unsigned short*)d_in[6]; P.w2 = (const unsigned short*)d_in[7];
    P.out = (unsigned short*)d_out; P.ws = (char*)d_ws;
    fused_block_kernel<<<256, 768, 0, stream>>>(P);
}
```
